```python
import torch

def _softmax(x, axis=-1):
    return torch.softmax(x, dim=axis)

def _gelu(x):
    return torch.nn.functional.gelu(x, approximate='tanh')

def _silu(x):
    return torch.nn.functional.silu(x)

def _sigmoid(x):
    return torch.sigmoid(x)

def _layernorm(x, axis=-1, eps=1e-05):
    m = x.mean(dim=axis, keepdim=True)
    v = x.var(dim=axis, keepdim=True, unbiased=False)
    return (x - m) / torch.sqrt(v + eps)

def _scatter_add(tgt, idx, src, axis=0):
    return tgt.clone().index_add_(axis, idx, src)

def _segment_sum(src, seg, n):
    out = torch.zeros((n,) + src.shape[1:], dtype=src.dtype)
    return out.index_add_(0, seg, src)

def _pad(x, pw):
    flat = tuple((v for pair in reversed(pw) for v in pair))
    return torch.nn.functional.pad(x, flat)
kernel_name = 'recurrent_scan__sliding_local_dilated__mixer_gmlp__prompt'

def setup_inputs():

    _w = lambda a: (lambda t: t.to(torch.bfloat16) if t.is_floating_point() else t)(torch.as_tensor(a))

    def _A_setup():
        B, S, D = (1, 2048, 512)
        return {'x': _w((lambda *s: torch.randn(*s, dtype=torch.bfloat16) * 0.05)(B, S, D)), 'wqkv': _w((lambda *s: torch.randn(*s, dtype=torch.bfloat16) * 0.05)(D, 3 * D)), 'wo': _w((lambda *s: torch.randn(*s, dtype=torch.bfloat16) * 0.05)(D, D))}
    _A_in = _A_setup()

    def _B_setup():
        B, S, D, F = (1, 2048, 512, 1376)
        Fh = F // 2
        return {'x': _w((lambda *s: torch.randn(*s, dtype=torch.bfloat16) * 0.05)(B, S, D)), 'w_in': _w((lambda *s: torch.randn(*s, dtype=torch.bfloat16) * 0.4)(D, 2 * Fh)), 'w_sp': _w(0.01 * (lambda *s: torch.randn(*s, dtype=torch.bfloat16) * 0.4)(S, S)), 'b_sp': _w(1.0 + 0.01 * (lambda *s: torch.randn(*s, dtype=torch.bfloat16) * 0.05)(S)), 'w_out': _w((lambda *s: torch.randn(*s, dtype=torch.bfloat16) * 0.05)(Fh, D))}
    _B_in = _B_setup()
    return {'A_x': _A_in['x'], 'A_wqkv': _A_in['wqkv'], 'A_wo': _A_in['wo'], 'B_x': _B_in['x'], 'B_w_in': _B_in['w_in'], 'B_w_sp': _B_in['w_sp'], 'B_b_sp': _B_in['b_sp'], 'B_w_out': _B_in['w_out']}

def reference(A_x, A_wqkv, A_wo, B_x, B_w_in, B_w_sp, B_b_sp, B_w_out):

    def _A_ref(x, wqkv, wo):
        B, S, D = x.shape
        H = 32
        Dh = D // H
        W, dil = (8, 2)
        qkv = (lambda x, *d: x.permute(*d))((x @ wqkv).reshape(B, S, 3, H, Dh), 2, 0, 3, 1, 4)
        q, k, v = (qkv[0], qkv[1], qkv[2])
        scale = Dh ** (-0.5)
        pos = torch.arange(S)
        off = torch.arange(W) * dil
        idx = pos[:, None] - off[None, :]
        mask = idx >= 0
        idx_c = torch.where(mask, idx, 0).reshape(-1)
        kg = k[:, :, idx_c, :].reshape(B, H, S, W, Dh)
        vg = v[:, :, idx_c, :].reshape(B, H, S, W, Dh)
        logits = torch.einsum('bhsd,bhswd->bhsw', q, kg) * scale
        logits = torch.where(mask[None, None, :, :], logits, -1000000000.0)
        attn = _softmax(logits, axis=-1)
        o = torch.einsum('bhsw,bhswd->bhsd', attn, vg)
        return (lambda x, *d: x.permute(*d))(o, 0, 2, 1, 3).reshape(B, S, D) @ wo

    def _B_ref(x, w_in, w_sp, b_sp, w_out):
        B, S, D = x.shape
        F = w_in.shape[-1]
        Fh = F // 2
        z = _gelu(torch.einsum('bsd,df->bsf', x, w_in))
        z1 = z[..., :Fh].clone()
        z2 = _layernorm(z[..., Fh:], axis=-1)
        g = torch.einsum('ts,bsf->btf', w_sp, z2) + b_sp[None, :, None]
        return torch.einsum('bsf,fd->bsd', z1 * g, w_out)
    x = A_x
    xn = _layernorm(x, axis=-1)
    s = _A_ref(xn, A_wqkv, A_wo)
    g = _silu(_B_ref(xn, B_w_in, B_w_sp, B_b_sp, B_w_out))
    return x + s * g
```

```cpp
#include <hip/hip_runtime.h>
#include <hip/hip_bf16.h>
typedef unsigned short u16;
__device__ __forceinline__ float bf2f(u16 v) { return __uint_as_float(((unsigned)v) << 16); }
__device__ __forceinline__ u16 f2bf(float f) { unsigned u = __float_as_uint(f); u += 0x7fffu + ((u >> 16) & 1u); return (u16)(u >> 16); }

__global__ void k_ln(const u16* in, int ld, int c0, int n, u16* out) {
  int r = blockIdx.x; __shared__ float red[256];
  float s = 0; for (int c = threadIdx.x; c < n; c += 256) s += bf2f(in[(size_t)r * ld + c0 + c]);
  red[threadIdx.x] = s; __syncthreads();
  for (int o = 128; o > 0; o >>= 1) { if (threadIdx.x < o) red[threadIdx.x] += red[threadIdx.x + o]; __syncthreads(); }
  float m = red[0] / n; __syncthreads();
  s = 0; for (int c = threadIdx.x; c < n; c += 256) { float d = bf2f(in[(size_t)r * ld + c0 + c]) - m; s += d * d; }
  red[threadIdx.x] = s; __syncthreads();
  for (int o = 128; o > 0; o >>= 1) { if (threadIdx.x < o) red[threadIdx.x] += red[threadIdx.x + o]; __syncthreads(); }
  float rs = rsqrtf(red[0] / n + 1e-5f);
  for (int c = threadIdx.x; c < n; c += 256) out[(size_t)r * n + c] = f2bf((bf2f(in[(size_t)r * ld + c0 + c]) - m) * rs);
}
__global__ void k_gemm(const u16* A, int lda, const u16* B, u16* C, int M, int N, int K, int act) {
  __shared__ float As[16][17], Bs[16][17];
  int tx = threadIdx.x & 15, ty = threadIdx.x >> 4;
  int row = blockIdx.y * 16 + ty, col = blockIdx.x * 16 + tx; float acc = 0;
  for (int k0 = 0; k0 < K; k0 += 16) {
    As[ty][tx] = (k0 + tx < K) ? bf2f(A[(size_t)row * lda + k0 + tx]) : 0.f;
    Bs[ty][tx] = (k0 + ty < K && col < N) ? bf2f(B[(size_t)(k0 + ty) * N + col]) : 0.f;
    __syncthreads();
    for (int k = 0; k < 16; ++k) acc += As[ty][k] * Bs[k][tx];
    __syncthreads();
  }
  if (act == 1) { float u = 0.7978845608028654f * (acc + 0.044715f * acc * acc * acc); acc = 0.5f * acc * (1.f + tanhf(u)); }
  if (col < N) C[(size_t)row * N + col] = f2bf(acc);
}
__global__ void k_attn(const u16* qkv, u16* o) {
  int s = blockIdx.x, h = threadIdx.x; if (h >= 32) return;
  float q[16]; for (int d = 0; d < 16; ++d) q[d] = bf2f(qkv[(size_t)s * 1536 + h * 16 + d]);
  float l[8]; float mx = -1e30f;
  for (int w = 0; w < 8; ++w) { int t = s - 2 * w; if (t >= 0) { float a = 0; for (int d = 0; d < 16; ++d) a += q[d] * bf2f(qkv[(size_t)t * 1536 + 512 + h * 16 + d]); l[w] = a * 0.25f; } else l[w] = -1e9f; mx = fmaxf(mx, l[w]); }
  float den = 0; for (int w = 0; w < 8; ++w) { l[w] = __expf(l[w] - mx); den += l[w]; }
  for (int d = 0; d < 16; ++d) { float a = 0; for (int w = 0; w < 8; ++w) { int t = s - 2 * w; if (t < 0) t = 0; a += l[w] * bf2f(qkv[(size_t)t * 1536 + 1024 + h * 16 + d]); } o[(size_t)s * 512 + h * 16 + d] = f2bf(a / den); }
}
__global__ void k_h(const u16* z, const u16* g, const u16* b, u16* h) {
  int t = blockIdx.x; for (int f = threadIdx.x; f < 688; f += 256) h[(size_t)t * 688 + f] = f2bf(bf2f(z[(size_t)t * 1376 + f]) * (bf2f(g[(size_t)t * 688 + f]) + bf2f(b[t])));
}
__global__ void k_fin(const u16* x, const u16* s, const u16* bo, u16* out, int n) {
  int i = blockIdx.x * 256 + threadIdx.x; if (i < n) { float g = bf2f(bo[i]); float si = g / (1.f + __expf(-g)); out[i] = f2bf(bf2f(x[i]) + bf2f(s[i]) * si); }
}
extern "C" void kernel_launch(void* const* d_in, const int* in_sizes, int n_in,
                              void* d_out, int out_size, void* d_ws, size_t ws_size,
                              hipStream_t stream) {
  const u16 *x = (const u16*)d_in[0], *wqkv = (const u16*)d_in[1], *wo = (const u16*)d_in[2], *win = (const u16*)d_in[4], *wsp = (const u16*)d_in[5], *bsp = (const u16*)d_in[6], *wout = (const u16*)d_in[7];
  u16* w = (u16*)d_ws;
  u16 *xn = w, *qkv = xn + 2048 * 512, *o = qkv + 2048 * 1536, *sA = o + 2048 * 512, *z = sA + 2048 * 512, *z2n = z + 2048 * 1376, *g = z2n + 2048 * 688, *h = g + 2048 * 688, *bo = h + 2048 * 688;
  k_ln<<<2048, 256, 0, stream>>>(x, 512, 0, 512, xn);
  k_gemm<<<dim3(96, 128), 256, 0, stream>>>(xn, 512, wqkv, qkv, 2048, 1536, 512, 0);
  k_attn<<<2048, 64, 0, stream>>>(qkv, o);
  k_gemm<<<dim3(32, 128), 256, 0, stream>>>(o, 512, wo, sA, 2048, 512, 512, 0);
  k_gemm<<<dim3(86, 128), 256, 0, stream>>>(xn, 512, win, z, 2048, 1376, 512, 1);
  k_ln<<<2048, 256, 0, stream>>>(z, 1376, 688, 688, z2n);
  k_gemm<<<dim3(43, 128), 256, 0, stream>>>(wsp, 2048, z2n, g, 2048, 688, 2048, 0);
  k_h<<<2048, 256, 0, stream>>>(z, g, bsp, h);
  k_gemm<<<dim3(32, 128), 256, 0, stream>>>(h, 688, wout, bo, 2048, 512, 688, 0);
  k_fin<<<4096, 256, 0, stream>>>(x, sA, bo, (u16*)d_out, 2048 * 512);
}
```

```cpp
#include <hip/hip_runtime.h>
#include <hip/hip_bf16.h>
#include <stdint.h>
#include <type_traits>
#include <utility>

#define LAS __attribute__((address_space(3)))
#define DEV __device__ __forceinline__
typedef short bf16x8 __attribute__((ext_vector_type(8)));
typedef short bf16x4 __attribute__((ext_vector_type(4)));
typedef float f32x4 __attribute__((ext_vector_type(4)));
typedef float f32x2 __attribute__((ext_vector_type(2)));
typedef unsigned u32x4 __attribute__((ext_vector_type(4)));
typedef unsigned u32x2 __attribute__((ext_vector_type(2)));
typedef int i32x4 __attribute__((ext_vector_type(4)));
typedef __bf16 bf2 __attribute__((ext_vector_type(2)));

constexpr unsigned WS_E = 0;
constexpr unsigned WS_FZ = 0xA0000;
constexpr unsigned WS_FO = 0xA8000;
constexpr unsigned WS_FH = 0xB0000;
constexpr unsigned WS_ST = 65536;
constexpr unsigned WS_Z = 1u << 20;
constexpr unsigned WS_O = 4u << 20;
constexpr unsigned WS_H = 6u << 20;
constexpr unsigned WS_PROF = 10u << 20;
constexpr unsigned WS_BYTES = 16u << 20;
constexpr unsigned OOB = 0x7fffff00u;

constexpr unsigned L_S0 = 81920;
constexpr unsigned L_MISC = 155648;
constexpr unsigned L_TOTAL = 163840;
constexpr unsigned Sslot(int g) { return L_S0 + (g % 3) * 24576; }
constexpr unsigned L_E = L_MISC + 512;
constexpr unsigned L_PF = L_MISC + 1024;
constexpr unsigned L_POLL = L_MISC + 1536;
constexpr unsigned L_XR = 147456;
constexpr unsigned XQ = 20480;
constexpr unsigned Wslot(int g) { return (g < 32 ? g % 4 : (g - 32) % 3) * XQ; }
constexpr unsigned Hoff(int K) {
  return K < 6 ? 57344 + K * 4096 : K < 8 ? 131072 + (K - 6) * 4096 : K < 14 ? 81920 + (K - 8) * 4096
       : K < 19 ? 20480 + (K - 14) * 4096 : K < 21 ? 139264 + (K - 19) * 4096 : 8192;
}

#define ROT (4 * (i & 3))
#define TROT (2 * (i & 7))
#define SPINL 20
constexpr int SPIN_LIMIT = 1 << SPINL;

#define SB __builtin_amdgcn_sched_barrier(0)

template <class F, int... I>
DEV void sfor_impl(F&& f, std::integer_sequence<int, I...>) { (f(std::integral_constant<int, I>{}), ...); }
template <int N, class F>
DEV void sfor(F&& f) { sfor_impl(f, std::make_integer_sequence<int, N>{}); }

DEV i32x4 mk_srd(const void* p, unsigned bytes) {
  uint64_t a = (uint64_t)p;
  i32x4 r;
  r.x = (int)(uint32_t)a; r.y = (int)((uint32_t)(a >> 32) & 0xffffu); r.z = (int)bytes; r.w = 0x00020000;
  return r;
}
DEV void set_m0(unsigned v) { asm volatile("s_mov_b32 m0, %0\n\ts_nop 0" ::"s"(v)); }
#define DMAF ""
#define LDF ""
template <int IMM>
DEV void dma(i32x4 srd, unsigned voff, unsigned soff) {
  asm volatile("buffer_load_dwordx4 %0, %1, %2 offen offset:%3" DMAF " lds" ::"v"(voff), "s"(srd), "s"(soff), "n"(IMM));
}
template <int IMM>
DEV void dma_c(i32x4 srd, unsigned voff, unsigned soff) {
  asm volatile("buffer_load_dwordx4 %0, %1, %2 offen offset:%3 sc1 lds" ::"v"(voff), "s"(srd), "s"(soff), "n"(IMM));
}
template <int IMM>
DEV void ld128(u32x4& d, i32x4 srd, unsigned voff, unsigned soff) {
  asm volatile("buffer_load_dwordx4 %0, %1, %2, %3 offen offset:%4" LDF : "=v"(d) : "v"(voff), "s"(srd), "s"(soff), "n"(IMM));
}
DEV void ld128_c(u32x4& d, i32x4 srd, unsigned voff, unsigned soff) {
  asm volatile("buffer_load_dwordx4 %0, %1, %2, %3 offen sc1" : "=v"(d) : "v"(voff), "s"(srd), "s"(soff));
}
DEV void ld64_c(u32x2& d, i32x4 srd, unsigned voff, unsigned soff) {
  asm volatile("buffer_load_dwordx2 %0, %1, %2, %3 offen sc1" : "=v"(d) : "v"(voff), "s"(srd), "s"(soff));
}
DEV void ld16(unsigned& d, i32x4 srd, unsigned voff, unsigned soff) {
  asm volatile("buffer_load_ushort %0, %1, %2, %3 offen" : "=v"(d) : "v"(voff), "s"(srd), "s"(soff));
}
template <int N> DEV void wait_vm() { asm volatile("s_waitcnt vmcnt(%0)" ::"n"(N) : "memory"); }
DEV void bar() { asm volatile("s_waitcnt lgkmcnt(0)\n\ts_barrier" ::: "memory"); }
template <int N> DEV void wait_bar() { asm volatile("s_waitcnt vmcnt(%0) lgkmcnt(0)\n\ts_barrier" ::"n"(N) : "memory"); }
template <class T> DEV void tie(T& v) { asm volatile("" : "+v"(v)); }
DEV unsigned opq(unsigned v) { asm volatile("" : "+v"(v)); return v; }

DEV unsigned pk2(float a, float b) {
  f32x2 v = {a, b};
  return __builtin_bit_cast(unsigned, __builtin_convertvector(v, bf2));
}
DEV float asf(unsigned u) { return __builtin_bit_cast(float, u); }
DEV unsigned asu(float f) { return __builtin_bit_cast(unsigned, f); }
DEV bf2 asbf2(unsigned u) { return __builtin_bit_cast(bf2, u); }
DEV float bflo(unsigned d) { return __builtin_bit_cast(float, d << 16); }
DEV float bfhi(unsigned d) { return __builtin_bit_cast(float, d & 0xffff0000u); }
template <int CTRL> DEV float dpp_add(float v) {
  return v + __builtin_bit_cast(float, __builtin_amdgcn_update_dpp(0, __builtin_bit_cast(int, v), CTRL, 0xf, 0xf, true));
}
DEV float row_sum16(float v) {
  v = dpp_add<0x128>(v); v = dpp_add<0x124>(v); v = dpp_add<0x122>(v); v = dpp_add<0x121>(v);
  return v;
}
DEV float x16_add(float v) { auto r = __builtin_amdgcn_permlane16_swap(__builtin_bit_cast(unsigned, v), __builtin_bit_cast(unsigned, v), false, false);
  return asf(r[0]) + asf(r[1]); }
DEV float x32_add(float v) { auto r = __builtin_amdgcn_permlane32_swap(__builtin_bit_cast(unsigned, v), __builtin_bit_cast(unsigned, v), false, false);
  return asf(r[0]) + asf(r[1]); }
DEV float x16_max(float v) { auto r = __builtin_amdgcn_permlane16_swap(__builtin_bit_cast(unsigned, v), __builtin_bit_cast(unsigned, v), false, false);
  return fmaxf(asf(r[0]), asf(r[1])); }
DEV float x32_max(float v) { auto r = __builtin_amdgcn_permlane32_swap(__builtin_bit_cast(unsigned, v), __builtin_bit_cast(unsigned, v), false, false);
  return fmaxf(asf(r[0]), asf(r[1])); }
DEV u32x4 swap_pack(unsigned alo, unsigned ahi, unsigned blo, unsigned bhi) {
  auto r0 = __builtin_amdgcn_permlane16_swap(alo, blo, false, false);
  auto r1 = __builtin_amdgcn_permlane16_swap(ahi, bhi, false, false);
  u32x4 o = {r0[0], r1[0], r0[1], r1[1]};
  return o;
}
DEV float gelu_t(float x) {
  float p = __builtin_fmaf(x * x, -0.10294324f, -2.3022082f);
  float e = __builtin_amdgcn_exp2f(x * p);
  return x * __builtin_amdgcn_rcpf(1.0f + e);
}
template <int AUX> DEV void st128(u32x4 d, __amdgpu_buffer_rsrc_t r, unsigned voff) { __builtin_amdgcn_raw_buffer_store_b128(d, r, voff, 0, AUX); }
DEV f32x4 mfma32(bf16x8 a, bf16x8 b, f32x4 c) { return __builtin_amdgcn_mfma_f32_16x16x32_bf16(a, b, c, 0, 0, 0); }
DEV f32x4 mfma16(u32x2 a, u32x2 b, f32x4 c) {
  return __builtin_amdgcn_mfma_f32_16x16x16bf16_1k(__builtin_bit_cast(bf16x4, a), __builtin_bit_cast(bf16x4, b), c, 0, 0, 0);
}
DEV bf16x8 lds128(LAS char* base, unsigned off) { return *(LAS const bf16x8*)(base + off); }
DEV bf16x8 ldstr(LAS char* base, unsigned off0, unsigned off1) {
  bf16x4 a = __builtin_amdgcn_ds_read_tr16_b64_v4i16((LAS bf16x4*)(base + off0));
  bf16x4 b = __builtin_amdgcn_ds_read_tr16_b64_v4i16((LAS bf16x4*)(base + off1));
  return __builtin_shufflevector(a, b, 0, 1, 2, 3, 4, 5, 6, 7);
}

#define NOB0
#define LNILP
#define PSI 7
#define PS 8
#define PZP 13
#define PO 24
#define PFA 0
#define HLP 0
#define T0 0
#define HLI 0
#define ZPR 0
#define PFH 33
#define PHP 35
#define JX 0
#define JP 0
#define NJ5 15
#define TM 15
#define OST 0
#define ZROT 4
#define TLBW 99
#define PB 3
#define PB2 108
#define PC 11
#define LPQ 4
#define LPW 0
#define LPB 99
#define LPB2 99
#define Z0P 0
#define CPF 1
#define PPW 0
#define PPB 0
#define ZE 8
#define ZX 0
#define XPR 0
#define PFW 4
#define Z2L 0
#define TORD 0
#define LPRIO 3
#define PZC 13
#define PFO 28
#define PXR 33
#define CPRIO 3
#define PFZ (PS + 3)
#define PIPE 0
#define GS 14
#define PSL 3
#define SH 2
constexpr int SHQ(int n) {
  constexpr int t[3][15] = {{0, 1, 2, 3, 4, 5, 6, 7, 8, 9, 10, 11, 12, 13, 14},
                            {0, 0, 1, 1, 2, 2, 3, 3, 4, 4, 5, 5, 5, 6, 7},
                            {0, 1, 2, 3, 4, 5, 5, 6, 7, 8, 8, 9, 10, 10, 11}};
  return t[SH][n];
}
#define PW 7

struct Sched { int wait[41]; };
constexpr Sched make_sched() {
  Sched s{};
  int need[41] = {};
  int n = 1 + CPF;
  for (int g = 0; g < 3 - Z2L; ++g) { n += 6; need[g] = n; if (g == 0 && TORD) n += 2 * (PFA == 1); }
  if (!TORD) n += 2 * (PFA == 1);
  n += PPW + PPB;
  s.wait[0] = n - need[0];
  for (int p = 0; p < 38; ++p) {
    if (p >= 1 - Z2L && p <= 13) { n += 6; need[p + 2] = n; }
    if (p == 10 || p == 12) n += 4;
    n += (p == LPQ) + (p == LPW) + (p == LPB) + (p == LPB2) + (p == TLBW);
    if (p >= 14 && p <= 29) { n += 6; need[p + 2] = n; }
    if (p == 14 || (p >= 16 && p <= 28)) n += 4;
    if (p >= 30 && p <= 33) { n += 8; need[p + 2] = n; }
    if (p == PXR) n += 2;
    if (p == 34 || p == 35) { n += 10; need[p + 2] = n; }
    if (p == 36) { n += 2; n += 8; need[38] = n; n += 3; }
    if (p == 37) { n += 6; need[39] = n; n += 5; need[40] = n; }
    s.wait[p + 1] = n - need[p + 1];
  }
  s.wait[39] = n - need[39];
  s.wait[40] = n - need[40];
  return s;
}
constexpr Sched SC = make_sched();
static_assert(SC.wait[0] <= 63 && SC.wait[1] <= 63 && SC.wait[2] <= 63 && SC.wait[3] <= 63, "vmcnt range");

#define TS(k)
#define TL(k)
#define TREADY()
#define TEXIT()
#define TFLUSH(p)
template <bool LG> DEV void lbar() { asm volatile("s_waitcnt lgkmcnt(0)\n\ts_barrier" ::: "memory"); }

DEV void touch(i32x4 srd, unsigned voff) {
  set_m0(L_PF);
  asm volatile("buffer_load_dword %0, %1, 0 offen lds" ::"v"(voff), "s"(srd));
}
DEV void poll_ld(i32x4 srd, unsigned voff, unsigned soff) {
  set_m0(L_POLL);
  asm volatile("buffer_load_dword %0, %1, %2 offen sc1 lds" ::"v"(voff), "s"(srd), "s"(soff));
}
template <int N> DEV void wait_tie(u32x2& a) { asm volatile("s_waitcnt vmcnt(%1)" : "+v"(a) : "n"(N) : "memory"); }
template <int N> DEV void wait_tie(u32x4& a, u32x4& b, u32x4& c, u32x4& d) {
  asm volatile("s_waitcnt vmcnt(%4)" : "+v"(a), "+v"(b), "+v"(c), "+v"(d) : "n"(N) : "memory");
}
template <int N> DEV void wait_tie(u32x4& a, u32x4& b) { asm volatile("s_waitcnt vmcnt(%2)" : "+v"(a), "+v"(b) : "n"(N) : "memory"); }
template <int N>
DEV void poll_wait(LAS char* L, i32x4 rWS, unsigned voff, unsigned soff, unsigned want) {
  wait_vm<N>();
  int it = 0;
  while (true) {
    unsigned v = *(LAS const volatile unsigned*)(L + L_POLL + (threadIdx.x & 63) * 4);
    if (__builtin_amdgcn_ballot_w64(v == want) == ~0ull || ++it > SPIN_LIMIT) break;
    __builtin_amdgcn_s_sleep(1);
    poll_ld(rWS, voff, soff);
    wait_vm<0>();
  }
}
DEV void poll_pipe(LAS char* L, i32x4 rWS, unsigned voff, unsigned soff, unsigned want) {
  auto ld = [&](unsigned slot) { set_m0(L_POLL + slot); asm volatile("buffer_load_dword %0, %1, %2 offen sc1 lds" ::"v"(voff), "s"(rWS), "s"(soff)); };
  ld(0); __builtin_amdgcn_s_sleep(PSL); ld(256); __builtin_amdgcn_s_sleep(PSL);
  unsigned slot = 0;
  for (int it = 0; it <= SPIN_LIMIT; ++it) {
    unsigned s2 = slot == 0 ? 512 : slot - 256;
    ld(s2);
    wait_vm<2>();
    unsigned v = *(LAS const volatile unsigned*)(L + L_POLL + slot + (threadIdx.x & 63) * 4);
    if (__builtin_amdgcn_ballot_w64(v == want) == ~0ull) break;
    slot = slot == 512 ? 0 : slot + 256;
    __builtin_amdgcn_s_sleep(PSL);
  }
}
template <int N> using IC = std::integral_constant<int, N>;

__global__ void __launch_bounds__(768, 3)
fused_block_kernel(const void* __restrict__ px, const void* __restrict__ pwqkv, const void* __restrict__ pwo,
                   const void* __restrict__ pwin, const void* __restrict__ pwsp, const void* __restrict__ pbsp,
                   const void* __restrict__ pwout, void* __restrict__ pout, void* __restrict__ pws) {
  LAS char* const L = (LAS char*)(size_t)0;
  const int tid = threadIdx.x;
  const int w = __builtin_amdgcn_readfirstlane(tid >> 6);
  const int l = tid & 63;
  const int bid = blockIdx.x, xcd = bid & 7, rr = bid >> 3;
  const int j = ((xcd & 1) ^ JP) * 4 + ((rr & 3) ^ JX), i = (xcd >> 1) * 8 + (rr >> 2);
  const int ustart = (43 * j) >> 3;
  const int nj = ((43 * (j + 1)) >> 3) - ustart;
  const i32x4 rX = mk_srd(px, 2048u * 1024u), rWS = mk_srd(pws, WS_BYTES);
  const __amdgpu_buffer_rsrc_t sWS = __builtin_amdgcn_make_buffer_rsrc(pws, 0, WS_BYTES, 0x00020000);
  const unsigned zsrc = WS_Z + j * 393216u;

  const unsigned zr = ZROT == 4 ? (i & 3) : ZROT == 2 ? 2 * (i & 1) : 0;
  uint64_t pc0; unsigned clen;
  asm volatile("s_getpc_b64 %0\n.Lfb_pc:\n\ts_mov_b32 %1, .Lfb_end-.Lfb_pc" : "=s"(pc0), "=s"(clen));

  if (w >= 8) {
    const int lw = w - 8;
    TL(41);
    const i32x4 rQKV = mk_srd(pwqkv, 512u * 3072u), rWO = mk_srd(pwo, 512u * 1024u);
    const i32x4 rWIN = mk_srd(pwin, 512u * 2752u), rWSP = mk_srd(pwsp, 2048u * 4096u), rWOUT = mk_srd(pwout, 688u * 1024u);
    const unsigned rot = ROT;
    unsigned vz[6];
    sfor<6>([&](auto E_) {
      constexpr int e = E_;
      unsigned n = (6 * lw + e) * 64 + l, row = n / 12u, pos = n - row * 12u;
      unsigned unit = (pos >> 1) ^ ((row >> 3) & 1u);
      vz[e] = row * 2752u + unit * 32u + (pos & 1u) * 16u - (e & 3) * 1024u;
      if constexpr (NJ5 & 1) vz[e] = (unit == 5 && nj == 5) ? OOB : vz[e];
    });
    const unsigned l6 = lw * 6144u, l4 = lw * 4096u;
    const unsigned zoff2 = 1376u + 32u * ustart, zoff1 = 32u * ustart;
    auto dmaZ = [&](auto S_) {
      constexpr int s = S_;
      sfor<6>([&](auto E_) { constexpr int e = E_;
        if constexpr ((e & 3) == 0) set_m0(Sslot(s) + l6 + e * 1024);
        dma<(e & 3) * 1024>(rWIN, vz[e], (s < 4 ? zoff2 : zoff1) + ((s + zr) & 3u) * (128u * 2752u)); });
    };
    if constexpr (CPF) {
      const unsigned per = (clen >> 12) + 1, ln = rr * per + (lw * 16 + (l & 15));
      touch(mk_srd((const void*)pc0, clen), ((l >> 4) == 0 && lw * 16 + l < per) ? ln * 128u : 0x7fffff00u);
    }
    u32x2 Ev;
    ld64_c(Ev, rWS, 0u, WS_E);
    auto pfA = [&]() {
      const unsigned jb = ((xcd & 1) ^ JP) * 4, row = 16 * rr + 4 * lw + (l >> 4);
      unsigned m = (l >> 2) & 3; m = m > 2 ? 2 : m;
      touch(rWIN, row * 2752u + ((l >> 3) & 1u) * 1376u + ((43 * jb) >> 3) * 32u + (l & 7) * 128u);
      touch(rQKV, row * 3072u + m * 1024u + jb * 128u + (l & 3) * 128u);
    };
    if constexpr (Z0P) __builtin_amdgcn_s_setprio(3);
    if constexpr (ZPR) __builtin_amdgcn_s_setprio(2);
    dmaZ(IC<0>{});
    if constexpr (Z0P) { SB; __builtin_amdgcn_s_setprio(0); }
    if constexpr (ZPR) { SB; __builtin_amdgcn_s_setprio(1); }
    if constexpr (PFA == 1 && TORD) pfA();
    dmaZ(IC<1>{});
    if constexpr (ZPR) { SB; __builtin_amdgcn_s_setprio(0); }
    if constexpr (!Z2L) dmaZ(IC<2>{});
    if constexpr (PFA == 1 && !TORD) pfA();
    if constexpr (PPW) {
      const unsigned lo = opq(l), row = 16 * rr + 4 * lw + (lo >> 4), jb = ((xcd & 1) ^ JP) * 4;
      touch(rWIN, row * 2752u + ((lo >> 3) & 1u) * 1376u + ((43 * jb) >> 3) * 32u + (lo & 7) * 128u);
    }
    sfor<PPB>([&](auto N_) { constexpr int n = N_;
      const unsigned lo = opq(l), row = 64u * i + 16u * (j & 3) + 4u * lw + (lo >> 4);
      touch(rWSP, row * 4096u + (((lo & 15) + 16 * n + 2 * (TROT)) & 31u) * 128u); });
    unsigned vw[4];
    auto mkvw = [&]() { const unsigned lo = opq(l);
      sfor<4>([&](auto E_) { constexpr int e = E_; unsigned row = 16 * lw + 4 * e + (lo >> 4), ch = (lo & 15) ^ (row & 15);
        vw[e] = (64u * i + row) * 4096u + ch * 16u - e * 1024u; }); };
    __builtin_amdgcn_s_setprio(LPRIO);
    wait_tie<SC.wait[0]>(Ev);
    const uint64_t e64 = (((uint64_t)(unsigned)__builtin_amdgcn_readfirstlane(Ev.y) << 32) | (uint32_t)__builtin_amdgcn_readfirstlane(Ev.x)) + 1;
    const unsigned elo = (unsigned)e64, ehi = (unsigned)(e64 >> 32);
    const u32x2 eflag = {elo, ehi};
    if (lw == 0 && l == 0) *(LAS u32x2*)(L + L_E) = eflag;
    TREADY();
    lbar<true>();
    TEXIT();

    unsigned vq[2], vlin, vzb, vzc, vo8, vo8o;
    auto dmaQ = [&](auto S_) {
      constexpr int s = S_;
      sfor<2>([&](auto H_) { constexpr int h = H_;
        set_m0(Sslot(s) + l6 + h * 3072);
        sfor<3>([&](auto E_) { constexpr int e = E_; dma<e * 1024>(rQKV, vq[h], (s - 8) * 64u * 3072u); }); });
    };
    auto dmaW = [&](auto G_) {
      constexpr int G = G_;
      set_m0(Wslot(G) + l4);
      sfor<4>([&](auto E_) { constexpr int e = E_; dma<e * 1024>(rWSP, vw[e], ((G - 16 + rot) & 15u) * 256u); });
    };
    auto dmaZN = [&](auto G_) {
      constexpr int G = G_;
      const unsigned so = zsrc + ((G - 16 + rot) & 15u) * 24576u;
      sfor<6>([&](auto E_) { constexpr int e = E_;
        if constexpr ((e & 3) == 0) set_m0(Sslot(G) + l6 + e * 1024);
        dma_c<(e & 3) * 1024>(rWS, e % 3 == 0 ? vlin : e % 3 == 1 ? vzb : vzc, so + (e & 4) * 1024); });
    };
    auto dmaWO = [&](auto G_) {
      constexpr int G = G_;
      set_m0(Wslot(G) + l4);
      sfor<4>([&](auto E_) { constexpr int e = E_; dma_c<e * 1024>(rWS, vlin, WS_O + i * 65536u + (G - 32) * 16384u - lw * 2048u); });
      set_m0(Sslot(G) + l4);
      sfor<4>([&](auto E_) { constexpr int e = E_;
        dma<e * 1024>(rWO, (e & 1) ? vo8o : vo8, ((G - 32) * 128u + 32u * lw + 8u * e) * 1024u - e * 1024u); });
    };
    auto dmaWOUT = [&](auto G_) {
      constexpr int G = G_;
      if constexpr (G == 38) {
        set_m0(Wslot(38) + lw * 2048u);
        sfor<2>([&](auto E_) { constexpr int e = E_;
          unsigned r0 = 640u + 16u * lw + 8u * e; r0 = r0 > 680u ? 680u : r0;
          dma<e * 1024>(rWOUT, e ? vo8o : vo8, r0 * 1024u - e * 1024u); });
      } else {
        set_m0(Wslot(G) + l4);
        sfor<4>([&](auto E_) { constexpr int e = E_;
          dma<e * 1024>(rWOUT, (e & 1) ? vo8o : vo8, ((G - 36) * 320u + 32u * lw + 8u * e) * 1024u - e * 1024u); });
        sfor<6>([&](auto E_) { constexpr int e = E_;
          if constexpr ((e & 3) == 0) set_m0(Sslot(G) + l6 + e * 1024);
          dma<(e & 3) * 1024>(rWOUT, (e & 1) ? vo8o : vo8, ((G - 36) * 320u + 128u + 48u * lw + 8u * e) * 1024u - (e & 3) * 1024u); });
      }
    };
    auto dmaH = [&](auto K_) {
      constexpr int K = K_;
      constexpr unsigned dst = Hoff(K);
      unsigned ch = 4u * K + lw; if constexpr (K == 21) ch = ch > 85u ? 85u : ch;
      set_m0(dst + lw * 1024u);
      dma_c<0>(rWS, vlin - l6, WS_H + i * 90112u + ch * 1024u);
    };
    sfor<38>([&](auto P_) {
      constexpr int p = P_;
      if constexpr (p == 5) {
        const unsigned lo = opq(l);
        sfor<2>([&](auto H_) { constexpr int h = H_;
          unsigned row = 8 * (2 * lw + h) + (lo >> 3), pos = lo & 7, swz = ((lo >> 4) & 1u) | (h << 1);
          vq[h] = row * 3072u + (((pos >> 1) ^ swz) * 32u) + (pos & 1u) * 16u +
            j * 128u; });
      }
      if constexpr (p == 4 || p == 8) { asm volatile("s_barrier" ::: "memory"); asm volatile("s_barrier" ::: "memory"); }
      if constexpr (p >= 1 - Z2L && p <= 5) dmaZ(IC<p + 2>{});
      if constexpr (p >= 6 && p <= 13) dmaQ(IC<p + 2>{});
      if constexpr (p == 9) mkvw();
      if constexpr (p == 10) {
        vlin = opq(l6 + l * 16u); dmaW(IC<16>{});
        const bool nar = (NJ5 & 2) && nj == 5;
        vzb = opq((nar && (l >> 4) == 1) ? OOB : vlin); vzc = opq((nar && (l >> 4) == 3) ? OOB : vlin);
      }
      if constexpr (p == 12) dmaW(IC<17>{});
      if constexpr (p >= 14 && p <= 29) dmaZN(IC<p + 2>{});
      if constexpr (p == 14) dmaW(IC<18>{});
      if constexpr (p >= 16 && p <= 28) dmaW(IC<p + 3>{});
      if constexpr (p == 28) {
        const unsigned lo = opq(l), pos = lo & 7;
        vo8 = (lo >> 3) * 1024u + (((pos >> 1) ^ ((lo >> 4) & 1u)) * 32u) + (pos & 1u) * 16u + j * 128u; vo8o = vo8 ^ 64u;
      }
      if constexpr (p >= 30 && p <= 33) dmaWO(IC<p + 2>{});
      if constexpr (p == PXR) {
        const unsigned lo = opq(l);
        set_m0(L_XR + lw * 2048u);
        sfor<2>([&](auto E_) { constexpr int e = E_; const unsigned row = 16 * lw + 8 * e + (lo >> 3);
          dma<e * 1024>(rX, (64u * i + row) * 1024u + j * 128u + (((lo & 7) ^ ((row >> 1) & 7u)) * 16u) - e * 1024u, 0u); });
      }
      if constexpr (p == 34 || p == 35) dmaWOUT(IC<p + 2>{});
      if constexpr (p == 36) {
        dmaWOUT(IC<38>{});
        sfor<8>([&](auto K_) { dmaH(K_); });
        sfor<3>([&](auto K_) { dmaH(IC<19 + K_>{}); });
      }
      if constexpr (p == 37) { sfor<11>([&](auto K_) { dmaH(IC<8 + K_>{}); }); }
      if constexpr (p == LPW) {
        const unsigned lo = opq(l), row = 16 * rr + 4 * lw + (lo >> 4), jb = ((xcd & 1) ^ JP) * 4;
        const unsigned a = row * 2752u + ((lo >> 3) & 1u) * 1376u + ((43 * jb) >> 3) * 32u + (lo & 7) * 128u;
        touch(rWIN, (((TM & 1) && (lo & 7) >= (jb ? 7u : 6u)) || ((TM & 8) && (lo & 8) && (ZROT || row < 384u))) ? OOB : a);
      }
      if constexpr (p == TLBW) {
        const unsigned lo = opq(l);
        const unsigned a = lw == 0 ? WS_Z + 3u * 393216u + 1280u : lw == 1 ? WS_H + 86u * 1024u : lw == 2 ? WS_H + 31u * 90112u + 86u * 1024u : (64u * i) * 1024u + j * 128u;
        touch(lw == 3 ? mk_srd(pout, 2048u * 1024u) : rWS, lo == 0 ? a : OOB);
      }
      if constexpr (p == LPQ) {
        const unsigned lo = opq(l), row = 16 * rr + 4 * lw + (lo >> 4), jb = ((xcd & 1) ^ JP) * 4;
        const unsigned m0_ = (lo >> 2) & 3, m = m0_ > 2 ? 2 : m0_;
        touch(rQKV, ((TM & 4) && m0_ > 2) ? OOB : row * 3072u + m * 1024u + jb * 128u + (lo & 3) * 128u);
      }
      sfor<2>([&](auto N_) { constexpr int n = N_;
        if constexpr (p == (n ? LPB2 : LPB)) { const unsigned lo = opq(l), row = 64u * i + 16u * (j & 3) + 4u * lw + (lo >> 4);
          touch(rWSP, row * 4096u + (((lo & 15) + 16 * n + 2 * (TROT)) & 31u) * 128u); } });
      TFLUSH(p);
      wait_vm<SC.wait[p + 1]>();
      TREADY();
      lbar<false>();
      TEXIT();
    });
    TFLUSH(38);
    wait_vm<SC.wait[39]>(); TREADY(); lbar<false>(); TEXIT(); TFLUSH(39);
    wait_vm<SC.wait[40]>(); TREADY(); lbar<false>(); TEXIT(); TFLUSH(40);
    wait_vm<0>();
    if constexpr (OST) asm volatile("s_barrier" ::: "memory");
    asm volatile(".Lfb_end:");
    return;
  }
  const i32x4 rBSP = mk_srd(pbsp, 4096u);
  const __amdgpu_buffer_rsrc_t sOUT = __builtin_amdgcn_make_buffer_rsrc(pout, 0, 2048u * 1024u, 0x00020000);
  const int g = l >> 4, c = l & 15, q = (l >> 2) & 3, p4 = l & 3;
  const int tt = w >> 1, fh = w & 1;
  const unsigned jb = ((xcd & 1) ^ JP) * 4;
  TS(0);
  unsigned bsp;
  u32x4 xh[2];
  auto ldhalo = [&]() { const unsigned lo = opq(l); int tok = 64 * i - 16 + 2 * w + (lo >> 5); tok = tok < 0 ? 0 : tok;
    unsigned vo = (unsigned)tok * 1024u + (lo & 31) * 16u;
    ld128<0>(xh[0], rX, vo, 0u); ld128<512>(xh[1], rX, vo, 0u); };
  {
    u32x4 xr[3][4];
    auto ldx = [&](int grp, u32x4 (&d)[4]) {
      int tok = 64 * i - 16 + 4 * grp + g;
      tok = tok < 0 ? 0 : tok;
      unsigned vo = (unsigned)tok * 1024u + c * 16u;
      ld128<0>(d[0], rX, vo, 0u); ld128<256>(d[1], rX, vo, 0u); ld128<512>(d[2], rX, vo, 0u); ld128<768>(d[3], rX, vo, 0u);
    };
    auto lnrow = [&](int grp, u32x4 (&d)[4]) {
      float s1 = 0.f, s2 = 0.f, t1 = 0.f, t2 = 0.f;
      sfor<16>([&](auto D_) {
        constexpr int k = D_;
        bf2 v = asbf2(d[k >> 2][k & 3]);
        if constexpr (k & 1) { t1 = __builtin_amdgcn_fdot2_f32_bf16(v, __builtin_bit_cast(bf2, 0x3f803f80u), t1, false); t2 = __builtin_amdgcn_fdot2_f32_bf16(v, v, t2, false); }
        else { s1 = __builtin_amdgcn_fdot2_f32_bf16(v, __builtin_bit_cast(bf2, 0x3f803f80u), s1, false); s2 = __builtin_amdgcn_fdot2_f32_bf16(v, v, s2, false); }
      });
      s1 = row_sum16(s1 + t1); s2 = row_sum16(s2 + t2);
      float mean = s1 * (1.f / 512.f);
      float var = fmaxf(__builtin_fmaf(s2, 1.f / 512.f, -mean * mean), 0.f);
      float rstd = __builtin_amdgcn_rsqf(var + 1e-5f), nb = -mean * rstd;
      int row = 4 * grp + g;
      unsigned a = row * 256u + ((c ^ (row & 15)) * 16u);
      sfor<4>([&](auto H_) {
        constexpr int h = H_;
        u32x4 o;
        f32x2 t[4];
        sfor<4>([&](auto D_) { constexpr int k = D_; unsigned v = d[h][k]; t[k] = f32x2{bflo(v), bfhi(v)}; });
        SB;
        sfor<4>([&](auto D_) { constexpr int k = D_; t[k] = __builtin_elementwise_fma(t[k], f32x2{rstd, rstd}, f32x2{nb, nb}); });
        SB;
        sfor<4>([&](auto D_) { constexpr int k = D_; o[k] = pk2(t[k][0], t[k][1]); });
        SB;
        *(LAS u32x4*)(L + a + h * XQ) = o;
      });
    };
    __builtin_amdgcn_s_setprio(3);
    if constexpr (T0) {
      if (T0 == 1 ? w >= 4 : w >= 6) {
        const unsigned lo = l, row = T0 == 1 ? 16 * rr + 4 * (w & 3) + (lo >> 4) : 16 * rr + 8 * (w & 1) + (lo >> 3);
        touch(mk_srd(pwin, 512u * 2752u), row * 2752u + (T0 == 1 ? ((lo >> 3) & 1u) : 1u) * 1376u + ((43 * jb) >> 3) * 32u + (lo & 7) * 128u);
      }
      SB;
    }
    if (w < 4) ldx(16 + w, xr[2]);
    ldx(w, xr[0]);
    ldx(8 + w, xr[1]);
    ld16(bsp, rBSP, (64 * i + 16 * tt + c) * 2u, 0u);
    SB;
    __builtin_amdgcn_s_setprio(0);
    TS(45);
    if (w < 4) { wait_tie<9>(xr[2][0], xr[2][1], xr[2][2], xr[2][3]);  lnrow(16 + w, xr[2]);  }
    wait_tie<5>(xr[0][0], xr[0][1], xr[0][2], xr[0][3]);

    lnrow(w, xr[0]);

    wait_tie<1>(xr[1][0], xr[1][1], xr[1][2], xr[1][3]);

    lnrow(8 + w, xr[1]);
    asm volatile("s_waitcnt vmcnt(0)" : "+v"(bsp)::"memory");
    if constexpr (PFA == 2) if (w >= PFW && w < PFW + 4) {
      const unsigned lo = opq(l), row = 16 * rr + 4 * (w - PFW) + (lo >> 4);
      unsigned m = (lo >> 2) & 3; m = m > 2 ? 2 : m;
      touch(mk_srd(pwin, 512u * 2752u), row * 2752u + ((lo >> 3) & 1u) * 1376u + ((43 * jb) >> 3) * 32u + (lo & 7) * 128u);
      touch(mk_srd(pwqkv, 512u * 3072u), row * 3072u + m * 1024u + jb * 128u + (lo & 3) * 128u);
    }
  }
  TS(1);
  bar();

  u32x2 ef;
  u32x4 G0, G1;
  auto ldstats = [&]() { const unsigned lo = opq(l), vst = (lo >> 3) * 1024u + (8 * w + (lo & 7)) * 16u, so = WS_ST + i * 16384u;
    ld128_c(G0, rWS, vst, so); ld128_c(G1, rWS, vst, so + 8192u); };
  auto pv = [&](unsigned m) { const unsigned lo = opq(l); return ((lo >> 1) & m) * 128u + (lo & 1) * 4u; };
  auto flag = [&](unsigned off) { if (w == PW) { __builtin_amdgcn_s_setprio(CPRIO); if (l == 0) __builtin_amdgcn_raw_buffer_store_b64(ef, sWS, off, 0, 16); __builtin_amdgcn_s_setprio(0); } };
  auto poll = [&](unsigned vo, unsigned so) { if (w == PW) { __builtin_amdgcn_s_setprio(CPRIO); poll_ld(rWS, vo, so); __builtin_amdgcn_s_setprio(0); } };
  auto pfB = [&](auto N_) { constexpr int n = N_; const unsigned lo = opq(l), row = 64u * i + 16u * (j & 3) + 4u * (w & 3) + (lo >> 4);
    touch(mk_srd(pwsp, 2048u * 4096u), w < 4 ? row * 4096u + (((lo & 15) + 16 * n + 2 * (TROT)) & 31u) * 128u : OOB); };
  auto post = [&](auto P_) {
    constexpr int P = P_;
    if constexpr (HLP != 0 && HLI != 0 && P == HLI) ldhalo();
    if constexpr (P == PFZ) flag(WS_FZ + (j * 32u + i) * 128u);
    if constexpr (P == PO) flag(WS_FO + (i * 8u + j) * 128u);
    if constexpr (P == PFH) flag(WS_FH + (i * 8u + j) * 128u);
    if constexpr (P == PSI) { __builtin_amdgcn_s_setprio(CPRIO); ldstats(); __builtin_amdgcn_s_setprio(0); }
    if constexpr (P == PZP) poll(pv(31), WS_FZ + j * 4096u);
    if constexpr (P == PFO) poll(pv(7), WS_FO + i * 1024u);
    if constexpr (P == PHP && !PIPE) poll(pv(7), WS_FH + i * 1024u);
  };
  auto pre = [&](auto P_) {
    constexpr int P = P_;
    if constexpr (HLP != 0 && P == HLP) {
      asm volatile("s_waitcnt vmcnt(0)" : "+v"(xh[0]), "+v"(xh[1]), "+v"(bsp)::"memory");
      float s1 = 0.f, s2 = 0.f, t1 = 0.f, t2 = 0.f;
      sfor<8>([&](auto D_) {
        constexpr int k = D_;
        bf2 v = asbf2(xh[k >> 2][k & 3]);
        if constexpr (k & 1) { t1 = __builtin_amdgcn_fdot2_f32_bf16(v, __builtin_bit_cast(bf2, 0x3f803f80u), t1, false); t2 = __builtin_amdgcn_fdot2_f32_bf16(v, v, t2, false); }
        else { s1 = __builtin_amdgcn_fdot2_f32_bf16(v, __builtin_bit_cast(bf2, 0x3f803f80u), s1, false); s2 = __builtin_amdgcn_fdot2_f32_bf16(v, v, s2, false); }
      });
      s1 = x16_add(row_sum16(s1 + t1)); s2 = x16_add(row_sum16(s2 + t2));
      float mean = s1 * (1.f / 512.f);
      float var = fmaxf(__builtin_fmaf(s2, 1.f / 512.f, -mean * mean), 0.f);
      float rstd = __builtin_amdgcn_rsqf(var + 1e-5f), nb = -mean * rstd;
      const unsigned lo = opq(l), row = 2 * w + (lo >> 5);
      const unsigned a = row * 256u + (((lo & 15) ^ row) * 16u) + ((lo >> 4) & 1u) * XQ;
      sfor<2>([&](auto H_) { constexpr int h = H_;
        u32x4 o;
        f32x2 t[4];
        sfor<4>([&](auto D_) { constexpr int k = D_; unsigned v = xh[h][k]; t[k] = f32x2{bflo(v), bfhi(v)}; });
        SB;
        sfor<4>([&](auto D_) { constexpr int k = D_; t[k] = __builtin_elementwise_fma(t[k], f32x2{rstd, rstd}, f32x2{nb, nb}); });
        SB;
        sfor<4>([&](auto D_) { constexpr int k = D_; o[k] = pk2(t[k][0], t[k][1]); });
        SB;
        *(LAS u32x4*)(L + a + 2 * h * XQ) = o;
      });
    }
    if constexpr (P == PB + 1) { if (w < 4) pfB(IC<0>{}); else if (PB2 == PB) pfB(IC<1>{}); }
    if constexpr (P == PB2 + 1 && PB2 != PB) { if (w < 4) pfB(IC<1>{}); }
    if constexpr (P == PC + 1) {
      const unsigned lo = opq(l);
      if (w == 0) touch(mk_srd(pwo, 512u * 1024u), (16u * rr + (lo >> 2)) * 1024u + jb * 128u + (lo & 3) * 128u);
      else if (w == 3) touch(rX, (64u * i + lo) * 1024u + j * 128u);
      else if (w < 3) { const unsigned r = 16u * (w - 1) + (lo >> 2);
        touch(mk_srd(pwout, 688u * 1024u), ((TM & 2) && r >= 22u) ? OOB : (22u * rr + r) * 1024u + jb * 128u + (lo & 3) * 128u); }
    }
    if constexpr (P == PS + 1) {
      wait_tie<PB2 == 108 ? 1 : 0>(G0, G1);
      int it = 0;
      while (true) {
        bool ok = G0.y == ef.x && G0.w == ef.x && G1.y == ef.x && G1.w == ef.x;
        if (__builtin_amdgcn_ballot_w64(ok) == ~0ull || ++it > SPIN_LIMIT) break;
        __builtin_amdgcn_s_sleep(1);
        ldstats();
        wait_tie<0>(G0, G1);
      }
      float s1 = asf(G0.x) + asf(G1.x), s2 = asf(G0.z) + asf(G1.z);
      s1 = x32_add(x16_add(dpp_add<0x128>(s1))); s2 = x32_add(x16_add(dpp_add<0x128>(s2)));
      float mean = s1 * (1.f / 688.f);
      float var = fmaxf(__builtin_fmaf(s2, 1.f / 688.f, -mean * mean), 0.f);
      float rstd = __builtin_amdgcn_rsqf(var + 1e-5f);
      f32x2 o = {rstd, -mean * rstd};
      if (l < 8) *(LAS f32x2*)(L + L_MISC + (8 * w + l) * 8) = o;
    }
    if constexpr (P == PZC + 1) { if (w == PW) poll_wait<0>(L, rWS, pv(31), WS_FZ + j * 4096u, (l & 1) ? ef.y : ef.x); }
    if constexpr (P == 30) { if (w == PW) poll_wait<0>(L, rWS, pv(7), WS_FO + i * 1024u, (l & 1) ? ef.y : ef.x); }
    if constexpr (P == 36) {
      if (w == PW) {
        if constexpr (PIPE) poll_pipe(L, rWS, pv(7), WS_FH + i * 1024u, (l & 1) ? ef.y : ef.x);
        else poll_wait<0>(L, rWS, pv(7), WS_FH + i * 1024u, (l & 1) ? ef.y : ef.x);
        if (bid == 0 && l == 0) __builtin_amdgcn_raw_buffer_store_b64(ef, sWS, WS_E, 0, 16);
      }
    }
  };

  const int tp = w >> 2, kh = (w >> 1) & 1;
  unsigned xa[2][2];
  sfor<4>([&](auto E_) { constexpr int t = E_ >> 1, e = E_ & 1;
    xa[t][e] = opq(c * 256u + (((4 * (2 * kh + e) + g) ^ c) * 16u) + (2 * tp + 1 + (kh ^ t)) * 4096u); });
  unsigned wa[3], wb[3];
  sfor<3>([&](auto U_) { constexpr int u = U_; wa[u] = (8 * g + q) * 192u + (((3 * fh + u) ^ (g & 1)) * 32u) + 8 * p4 + kh * 12288u; });
  const int hh = w >> 1, th = w & 1;

  bf16x8 xf[2][8];
  bf16x8 wf[2][3];
  f32x4 a2[2][3] = {}, a1[2][3] = {};
  auto RZx = [&](auto N_, auto T_) { constexpr int n = N_, t = T_;
    if constexpr (t == 0 && (n & 1) == 0) sfor<3>([&](auto U_) { constexpr int u = U_; wb[u] = opq(wa[u] + Sslot(n >> 1)); });
    if constexpr (n < 8) {
      if constexpr (ZROT) xf[t][n] = lds128(L, xa[t][n & 1] + (((n >> 1) + zr) & 3u) * XQ);
      else xf[t][n] = lds128(L, xa[t][n & 1] + (n >> 1) * XQ);
    } };
  auto RZw = [&](auto N_, auto U_) { constexpr int n = N_, u = U_, e = n & 1;
    wf[e][u] = ldstr(L, wb[u] + e * 6144, wb[u] + e * 6144 + 768); };
  bf16x8 qx[2][3], qw[2][3];
  f32x4 aq[2] = {}, ak[3] = {}, av[3] = {};
  unsigned qa, qb_, xq[4];
  { unsigned swz = ((q >> 1) & 1u) | ((g & 1u) << 1); qa = g * 3072u + q * 128u + ((hh ^ swz) * 32u) + 8 * p4; }
  auto RQ = [&](auto Q_, auto R_) {
    constexpr int Q = Q_, r = R_, s = 8 + (Q >> 1), ks = Q & 1, b = Q & 1;
    if constexpr (r == 0 && Q == 0) { const unsigned co = opq(c); sfor<4>([&](auto K_) { constexpr int k = K_; xq[k] = opq(co * 256u + (((4 * k + g) ^ co) * 16u) + th * 8192u); }); }
    if constexpr (r == 0 && ks == 0) qb_ = opq(qa + Sslot(s));
    constexpr int m = r == 0 ? 0 : r == 3 ? 1 : r == 5 ? 2 : -1;
    constexpr int t = r == 1 ? 1 : r == 2 ? 2 : r == 4 ? 0 : -1;
    if constexpr (m >= 0) qw[b][m] = ldstr(L, qb_ + ks * 12288 + m * 1024, qb_ + ks * 12288 + m * 1024 + 512);
    else qx[b][t] = lds128(L, xq[Q & 3] + (Q >> 2) * XQ + t * 4096);
  };

  float z2v[3][4];
  const bool valid2 = (fh == 0) || (nj == 6);
  auto norm_store = [&]() {
    f32x4 f0 = *(LAS const f32x4*)(L + L_MISC + (16 * tt + 4 * g) * 8), f1 = *(LAS const f32x4*)(L + L_MISC + (16 * tt + 4 * g) * 8 + 16);
    float rs[4] = {f0[0], f0[2], f1[0], f1[2]}, nb[4] = {f0[1], f0[3], f1[1], f1[3]};
    unsigned lo[3], hi[3];
    sfor<3>([&](auto U_) { constexpr int u = U_;
      lo[u] = pk2(__builtin_fmaf(z2v[u][0], rs[0], nb[0]), __builtin_fmaf(z2v[u][1], rs[1], nb[1]));
      hi[u] = pk2(__builtin_fmaf(z2v[u][2], rs[2], nb[2]), __builtin_fmaf(z2v[u][3], rs[3], nb[3])); });
    u32x4 o01 = swap_pack(lo[0], hi[0], lo[1], hi[1]);
    u32x4 o2 = swap_pack(lo[2], hi[2], lo[2], hi[2]);
    unsigned so = zsrc + (8 * i + 2 * tt) * 1536u + 3 * fh * 256u;
    __builtin_amdgcn_s_setprio(CPRIO);
    st128<16>(o01, sWS, (g >> 1) * 1536u + ((g & 1) * 16 + c) * 16u + so);
    if (!(NJ5 & 4) || valid2) st128<16>(o2, sWS, (g >> 1) * 1536u + (32 + c) * 16u + so);
    SB;
    __builtin_amdgcn_s_setprio(0);
  };

  RZx(IC<0>{}, IC<0>{}); RZw(IC<0>{}, IC<0>{}); RZx(IC<0>{}, IC<1>{}); RZw(IC<0>{}, IC<1>{}); RZw(IC<0>{}, IC<2>{});
  SB;
  const unsigned zsw = opq(Sslot(3) + w * 3072u + l * 16u);
  sfor<16>([&](auto N_) {
    constexpr int n = N_, b = n & 1, m = n & 7;
    constexpr int per = (n + 1) >> 1;
    if constexpr (b == 1) {
      if constexpr (n < 8) sfor<6>([&](auto I_) { tie(a2[I_ / 3][I_ % 3]); }); else sfor<6>([&](auto I_) { tie(a1[I_ / 3][I_ % 3]); });
      pre(IC<per>{});
      bar();
      post(IC<per>{});
    }
    auto M = [&](auto T_, auto U_) { constexpr int t = T_, u = U_;
      if constexpr (n < 8) a2[t][u] = mfma32(xf[t][m], wf[b][u], a2[t][u]); else a1[t][u] = mfma32(wf[b][u], xf[t][m], a1[t][u]); SB; };
    using N = IC<n + 1>;
    M(IC<1>{}, IC<0>{});
    if constexpr (n + 1 < 16) { RZx(N{}, IC<0>{}); RZw(N{}, IC<0>{}); } else { RQ(IC<0>{}, IC<0>{}); RQ(IC<0>{}, IC<1>{}); }
    SB;
    M(IC<1>{}, IC<1>{});
    if constexpr (n + 1 < 16) RZx(N{}, IC<1>{}); else RQ(IC<0>{}, IC<2>{});
    SB;
    M(IC<1>{}, IC<2>{});
    if constexpr (n + 1 < 16) RZw(N{}, IC<1>{}); else RQ(IC<0>{}, IC<3>{});
    SB;
    M(IC<0>{}, IC<0>{});
    if constexpr (n + 1 < 16) RZw(N{}, IC<2>{}); else { RQ(IC<0>{}, IC<4>{}); RQ(IC<0>{}, IC<5>{}); }
    SB;
    M(IC<0>{}, IC<1>{});
    if constexpr (n == 7) { sfor<3>([&](auto U_) { constexpr int u = U_; *(LAS f32x4*)(L + zsw + u * 1024) = a2[1][u]; }); SB; }
    M(IC<0>{}, IC<2>{});
    if constexpr (n == ZE) {
      if constexpr (ZE == 8) sfor<6>([&](auto I_) { tie(a1[I_ / 3][I_ % 3]); });
      TS(46);
      bar();
      TS(47);
      f32x4 zo[3];
      const unsigned sr_ = opq(Sslot(3) + (w ^ 2) * 3072u + l * 16u);
      sfor<3>([&](auto U_) { constexpr int u = U_; zo[u] = *(LAS const f32x4*)(L + sr_ + u * 1024); });
      bar();
      TS(48);
      ef = *(LAS const u32x2*)(L + L_E);
      const unsigned tag = ef.x;
      float s1[4], s2[4];
      sfor<4>([&](auto R_) { constexpr int r = R_;
        sfor<3>([&](auto U_) { constexpr int u = U_; z2v[u][r] = gelu_t(a2[0][u][r] + zo[u][r]); });
        float t = valid2 ? z2v[2][r] : 0.f;
        s1[r] = row_sum16(z2v[0][r] + z2v[1][r] + t);
        s2[r] = row_sum16(__builtin_fmaf(z2v[0][r], z2v[0][r], __builtin_fmaf(z2v[1][r], z2v[1][r], t * t)));
      });
      sfor<4>([&](auto R_) { constexpr int r = R_; tie(s1[r]); tie(s2[r]); });
      const bool c1 = c & 1, c2 = c & 2;
      float m1a = c1 ? s1[1] : s1[0], m1b = c1 ? s1[3] : s1[2], m2a = c1 ? s2[1] : s2[0], m2b = c1 ? s2[3] : s2[2];
      float m1 = c2 ? m1b : m1a, m2 = c2 ? m2b : m2a;
      u32x4 gr = {__builtin_bit_cast(unsigned, m1), tag, __builtin_bit_cast(unsigned, m2), tag};
      TS(49);
      __builtin_amdgcn_s_setprio(CPRIO);
      if (c < 4)
        st128<16>(gr, sWS, (16 * tt + 4 * g + c) * 16u + WS_ST + (i * 16u + 2 * j + fh) * 1024u);
      SB;
      __builtin_amdgcn_s_setprio(0);
      TS(50);
    }
    if constexpr (n == 15) {
      const unsigned sw_ = opq(Sslot(7) + w * 3072u + l * 16u), sr_ = opq(Sslot(7) + (w ^ 2) * 3072u + l * 16u);
      sfor<3>([&](auto U_) { constexpr int u = U_; *(LAS f32x4*)(L + sw_ + u * 1024) = a1[1][u]; });
      bar();
      if constexpr (PB2 == 108) pfB(IC<1>{});
      f32x4 zo[3];
      sfor<3>([&](auto U_) { constexpr int u = U_; zo[u] = *(LAS const f32x4*)(L + sr_ + u * 1024); });
      bar();
      sfor<3>([&](auto U_) { constexpr int u = U_; a1[0][u] += zo[u]; tie(a1[0][u]); });
    }
  });
  unsigned sa[2];
  sfor<2>([&](auto E_) { constexpr int e = E_; sa[e] = opq((32 * tp + c) * 256u + (((g ^ (c & 3)) + 4 * ((2 * kh + e) ^ (c >> 2))) * 16u)); });
  unsigned sa3[2];
  sfor<2>([&](auto E_) { constexpr int e = E_; sa3[e] = opq(sa[e] + Wslot(19)); });
  const unsigned za = (8 * kh + g) * 1536u + (48 * fh + c) * 16u;
  unsigned zb;
  bf16x8 sw[2][2], sz[2][3];
  f32x4 ag[2][3] = {};
  auto RS = [&](auto Q_, auto R_) {
    constexpr int Q = Q_, r = R_, G = 16 + (Q >> 1), e = Q & 1, b = Q & 1;
    if constexpr (r == 0 && e == 0) zb = opq(za + Sslot(G));
    if constexpr (r == 1 || r == 2) sw[b][r - 1] = (G & 3) == 3 ? lds128(L, sa3[e] + (r - 1) * 4096) : lds128(L, sa[e] + Wslot(G) + (r - 1) * 4096);
    else { constexpr int u = r == 0 ? 0 : r - 2; sz[b][u] = lds128(L, zb + e * 6144 + u * 256); }
  };
  if constexpr (PS + 1 == 8) norm_store();
  sfor<16>([&](auto Q_) {
    constexpr int Q = Q_, b = Q & 1;
    constexpr int per = 8 + ((Q + 1) >> 1), ph = (Q + 1) & 1;
    if constexpr (ph == 0) {
      tie(aq[0]); tie(aq[1]); tie(ak[0]); tie(ak[1]); tie(ak[2]); tie(av[0]); tie(av[1]); tie(av[2]);
      pre(IC<per>{});
      if constexpr (per == PFZ) wait_bar<0>(); else bar();
      post(IC<per>{});
      if constexpr (per == PS + 1) norm_store();
    }
    auto R = [&](auto R_) { if constexpr (Q + 1 < 16) RQ(IC<Q + 1>{}, R_); else if constexpr (R_ < 5) RS(IC<0>{}, R_); SB; };
    aq[0] = mfma32(qw[b][0], qx[b][1], aq[0]); SB;
    aq[1] = mfma32(qw[b][0], qx[b][2], aq[1]); SB;
    R(IC<0>{});
    ak[0] = mfma32(qw[b][1], qx[b][0], ak[0]); SB;
    R(IC<1>{});
    ak[1] = mfma32(qw[b][1], qx[b][1], ak[1]); SB;
    R(IC<2>{});
    ak[2] = mfma32(qw[b][1], qx[b][2], ak[2]); SB;
    R(IC<3>{});
    av[0] = mfma32(qx[b][0], qw[b][2], av[0]); SB;
    R(IC<4>{});
    av[1] = mfma32(qx[b][1], qw[b][2], av[1]); SB;
    R(IC<5>{});
    av[2] = mfma32(qx[b][2], qw[b][2], av[2]); SB;
  });

  u32x2 qb[2], kb[3], vb[3], Pc[2], Pp[2];
  f32x4 Sc[2], Sp[2], Ov[2];
  float lg[2][2], mx[2];
  float z1v[3][4];
  const bool par = c & 1;
  const bool cur0 = 4 * g + (c & 1) <= c, cur1 = 4 * g + 2 + (c & 1) <= c;
  float pe[2][3];
  auto at_sel = [&](auto A_) { constexpr int a = A_;
    const bool noprev = (a == 0) && (i == 0) && (th == 0);
    f32x4 sc = Sc[a], sp = Sp[a];
    float c0 = sc[0], c1 = sc[1], c2 = sc[2], c3 = sc[3], p0 = sp[0], p1 = sp[1], p2 = sp[2], p3 = sp[3];
    float vc0 = par ? c1 : c0, vp0 = par ? p1 : p0, vc1 = par ? c3 : c2, vp1 = par ? p3 : p2;
    vp0 = noprev ? -4.0e9f : vp0; vp1 = noprev ? -4.0e9f : vp1;
    lg[a][0] = cur0 ? vc0 : vp0; lg[a][1] = cur1 ? vc1 : vp1;
    tie(lg[a][0]); tie(lg[a][1]); };
  auto at_max = [&](auto A_) { constexpr int a = A_;
    mx[a] = x32_max(x16_max(fmaxf(lg[a][0], lg[a][1])));
    tie(mx[a]); };
  auto at_exp = [&](auto A_) { constexpr int a = A_;
    const float sc = 0.25f * 1.4426950408889634f;
    pe[a][0] = __builtin_amdgcn_exp2f((lg[a][0] - mx[a]) * sc); pe[a][1] = __builtin_amdgcn_exp2f((lg[a][1] - mx[a]) * sc);
    pe[a][2] = x32_add(x16_add(pe[a][0] + pe[a][1]));
    tie(pe[a][0]); tie(pe[a][1]); tie(pe[a][2]); };
  auto at_p = [&](auto A_) { constexpr int a = A_;
    const float inv = __builtin_amdgcn_rcpf(pe[a][2]);
    unsigned pw = pk2(pe[a][0] * inv, pe[a][1] * inv);
    unsigned w0 = par ? (pw << 16) : (pw & 0xffffu), w1 = par ? (pw & 0xffff0000u) : (pw >> 16);
    Pc[a] = u32x2{cur0 ? w0 : 0u, cur1 ? w1 : 0u}; Pp[a] = u32x2{cur0 ? 0u : w0, cur1 ? 0u : w1};
    tie(Pc[a]); tie(Pp[a]); };
  auto piece = [&](auto N_) {
    constexpr int n = N_;
    const f32x4 zero = {};
    auto cvt = [&](const f32x4& v, u32x2& d) { d = u32x2{pk2(v[0], v[1]), pk2(v[2], v[3])}; tie(d); };
    if constexpr (n == 0) { cvt(aq[0], qb[0]); cvt(aq[1], qb[1]); }
    else if constexpr (n == 1) { cvt(ak[0], kb[0]); cvt(ak[1], kb[1]); cvt(ak[2], kb[2]); }
    else if constexpr (n == 2) { Sc[0] = mfma16(kb[1], qb[0], zero); Sp[0] = mfma16(kb[0], qb[0], zero); tie(Sc[0]); tie(Sp[0]); cvt(av[0], vb[0]); cvt(av[1], vb[1]); }
    else if constexpr (n == 3) { Sc[1] = mfma16(kb[2], qb[1], zero); Sp[1] = mfma16(kb[1], qb[1], zero); tie(Sc[1]); tie(Sp[1]); cvt(av[2], vb[2]); }
    else if constexpr (n == 4) at_sel(IC<0>{});
    else if constexpr (n == 5) at_max(IC<0>{});
    else if constexpr (n == 6) at_sel(IC<1>{});
    else if constexpr (n == 7) at_max(IC<1>{});
    else if constexpr (n == 8) at_exp(IC<0>{});
    else if constexpr (n == 9) at_p(IC<0>{});
    else if constexpr (n == 10) at_exp(IC<1>{});
    else if constexpr (n == 11) at_p(IC<1>{});
    else if constexpr (n == 12) { Ov[0] = mfma16(vb[1], Pc[0], zero); Ov[0] = mfma16(vb[0], Pp[0], Ov[0]); tie(Ov[0]); }
    else if constexpr (n == 13) { Ov[1] = mfma16(vb[2], Pc[1], zero); Ov[1] = mfma16(vb[1], Pp[1], Ov[1]); tie(Ov[1]); }
    else {
      u32x4 ov = swap_pack(pk2(Ov[0][0], Ov[0][1]), pk2(Ov[0][2], Ov[0][3]), pk2(Ov[1][0], Ov[1][1]), pk2(Ov[1][2], Ov[1][3]));
      __builtin_amdgcn_s_setprio(CPRIO);
      st128<16>(ov, sWS, (g >> 1) * 1024u + (16 * (g & 1) + c) * 16u + WS_O + i * 65536u + (2 * (4 * j + hh)) * 1024u + th * 512u);
      SB;
      __builtin_amdgcn_s_setprio(0);
    }
  };
  auto shadow = [&](auto Q_) {
    constexpr int Q = Q_;
    sfor<15>([&](auto N_) { constexpr int n = N_; if constexpr (SHQ(n) == Q) piece(N_); });
    if constexpr (Q >= GS && Q < GS + 12) {
      constexpr int n = Q - GS, u = n >> 2, r = n & 3;
      z1v[u][r] = gelu_t(a1[0][u][r]); tie(z1v[u][r]);
    }
  };
  sfor<32>([&](auto Q_) {
    constexpr int Q = Q_, b = Q & 1;
    constexpr int per = 16 + ((Q + 1) >> 1), ph = (Q + 1) & 1;
    if constexpr (ph == 0 && Q != 31) {
      tie(ag[0][0]); tie(ag[0][1]); tie(ag[0][2]); tie(ag[1][0]); tie(ag[1][1]); tie(ag[1][2]);
      pre(IC<per>{});
      if constexpr (per == PO) wait_bar<0>(); else bar();
      post(IC<per>{});
    }
    auto R = [&](auto R_) { if constexpr (Q + 1 < 32) { RS(IC<Q + 1>{}, R_); SB; } };
    ag[0][0] = mfma32(sz[b][0], sw[b][0], ag[0][0]); SB;
    R(IC<0>{}); R(IC<1>{});
    ag[1][0] = mfma32(sz[b][0], sw[b][1], ag[1][0]); SB;
    R(IC<2>{});
    ag[0][1] = mfma32(sz[b][1], sw[b][0], ag[0][1]); SB;
    R(IC<3>{});
    ag[1][1] = mfma32(sz[b][1], sw[b][1], ag[1][1]); SB;
    R(IC<4>{});
    ag[0][2] = mfma32(sz[b][2], sw[b][0], ag[0][2]); SB;
    shadow(Q_); SB;
    ag[1][2] = mfma32(sz[b][2], sw[b][1], ag[1][2]); SB;
  });
  auto scr_of = [&](unsigned v) { return v < 4 ? v * XQ + 16384u : (v < 6 ? Sslot(0) - 4 * 3072u : Sslot(2) - 6 * 3072u) + 16384u + v * 3072u; };
  const unsigned scr = opq(scr_of(w) + l * 16u), scr2 = opq(scr_of(w ^ 2) + l * 16u);
  sfor<3>([&](auto U_) { constexpr int u = U_;
    f32x4 t0 = ag[0][u], t1 = ag[1][u]; *(LAS f32x4*)(L + scr + u * 1024) = kh ? t0 : t1; });
  bar();

  const int dq = w & 3;
  const unsigned orow = (64u * i + 32 * tp + 16 * (g & 1) + c) * 1024u + (64 * j + 16 * dq + 8 * (g >> 1)) * 2u;
  const unsigned oa = opq(g * 1024u + (32 * tp + c) * 16u);
  unsigned ta;
  { unsigned swz = ((q >> 1) & 1u) | ((g & 1u) << 1); ta = (8 * g + q) * 128u + ((dq ^ swz) * 32u) + 8 * p4; }
  unsigned tb;
  bf16x8 of[2][2], wof[2];
  f32x4 as[2] = {};
  auto RO = [&](auto Q_) {
    constexpr int Q = Q_, G = 32 + (Q >> 2), ks = Q & 3, b = Q & 1;
    if constexpr (ks == 0) tb = opq(ta + Sslot(G));
    wof[b] = ldstr(L, tb + ks * 4096, tb + ks * 4096 + 512);
    sfor<2>([&](auto B_) { constexpr int t = B_; of[b][t] = lds128(L, oa + Wslot(G) + ks * 4096 + t * 256); });
  };
  {
    float bb = bflo(bsp);
    unsigned lo[3], hi[3];
    sfor<3>([&](auto U_) { constexpr int u = U_;
      f32x4 o = *(LAS const f32x4*)(L + scr2 + u * 1024);
      f32x4 t0 = ag[0][u], t1 = ag[1][u]; f32x4 m = kh ? t1 : t0;
      float h0 = z1v[u][0] * (m[0] + o[0] + bb), h1 = z1v[u][1] * (m[1] + o[1] + bb);
      float h2 = z1v[u][2] * (m[2] + o[2] + bb), h3 = z1v[u][3] * (m[3] + o[3] + bb);
      lo[u] = pk2(h0, h1); hi[u] = pk2(h2, h3); });
    u32x4 o01 = swap_pack(lo[0], hi[0], lo[1], hi[1]);
    u32x4 o2 = swap_pack(lo[2], hi[2], lo[2], hi[2]);
    unsigned so = WS_H + i * 90112u + (ustart + 3 * fh) * 2048u + tt * 256u;
    __builtin_amdgcn_s_setprio(CPRIO);
    st128<16>(o01, sWS, ((g & 1) * 2 + (g >> 1)) * 1024u + c * 16u + so);
    unsigned so2 = valid2 ? so + 4096u : WS_H + i * 90112u + 86u * 1024u + tt * 256u;
    if (!(NJ5 & 8) || valid2) st128<16>(o2, sWS, (g >> 1) * 1024u + c * 16u + so2);
    SB;
    __builtin_amdgcn_s_setprio(0);
  }
  RO(IC<0>{});
  SB;
  sfor<16>([&](auto Q_) {
    constexpr int Q = Q_, b = Q & 1;
    constexpr int per = 32 + ((Q + 1) >> 2), ph = (Q + 1) & 3;
    if constexpr (ph == 0) {
      tie(as[0]); tie(as[1]);
      pre(IC<per>{});
      if constexpr (per == PFH) wait_bar<0>(); else bar();
      post(IC<per>{});
    }
    as[0] = mfma32(wof[b], of[b][0], as[0]); SB;
    if constexpr (Q + 1 < 16) RO(IC<Q + 1>{});
    SB;
    as[1] = mfma32(wof[b], of[b][1], as[1]); SB;
  });
  bf16x8 wo[22];
  auto RW = [&](auto K_) {
    constexpr int K = K_;
    constexpr int G = K < 10 ? 36 : K < 20 ? 37 : 38, kk = K < 10 ? K : K < 20 ? K - 10 : K - 20;
    if constexpr (kk == 0) tb = opq(ta + Wslot(G));
    if constexpr (kk == 4) tb = opq(ta + Sslot(G));
    constexpr unsigned off = (kk < 4 ? kk : kk - 4) * 4096;
    wo[K] = ldstr(L, tb + off, tb + off + 512);
  };
  sfor<10>([&](auto K_) { RW(K_); });
  tie(as[0]); tie(as[1]);
  bar();
  sfor<10>([&](auto K_) { RW(IC<10 + K_>{}); });
  bar();
  RW(IC<20>{}); RW(IC<21>{});
  f32x4 ab[2] = {};
  bf16x8 hf[2][2];
  unsigned ha0 = opq(oa + 57344u), ha2 = opq(oa + 131072u);
  auto RH = [&](auto N_) {
    constexpr int n = N_, K = n < 14 ? n : n < 17 ? n + 5 : n - 3, b = n & 1;
    constexpr unsigned o = Hoff(K);
    if constexpr (o >= 131072) { hf[b][0] = lds128(L, ha2 + (o - 131072)); hf[b][1] = lds128(L, ha2 + (o - 131072) + 256); }
    else if constexpr (o >= 57344) { hf[b][0] = lds128(L, ha0 + (o - 57344)); hf[b][1] = lds128(L, ha0 + (o - 57344) + 256); }
    else { hf[b][0] = lds128(L, oa + o); hf[b][1] = lds128(L, oa + o + 256); }
  };
  RH(IC<0>{});
  if (g >= 2) wo[21] = bf16x8{};
  SB;
  sfor<22>([&](auto N_) {
    constexpr int n = N_, K = n < 14 ? n : n < 17 ? n + 5 : n - 3, b = n & 1;
    if constexpr (n == 7 || n == 16) { tie(ab[0]); tie(ab[1]); bar(); }
    ab[0] = mfma32(wo[K], hf[b][0], ab[0]); SB;
    if constexpr (n + 1 < 22) RH(IC<n + 1>{});
    SB;
    ab[1] = mfma32(wo[K], hf[b][1], ab[1]); SB;
  });
  TS(43);
  {
    wait_vm<0>();
    const unsigned xrow = 32 * tp + 16 * (g & 1) + c;
    const unsigned xa_ = opq(L_XR + xrow * 128u + (((2 * dq + (g >> 1)) ^ ((xrow >> 1) & 7u)) * 16u));
    const u32x4 xres = *(LAS const u32x4*)(L + xa_);
    float y[2][4];
    sfor<2>([&](auto B_) { constexpr int t = B_; sfor<4>([&](auto R_) { constexpr int r = R_;
      float B = ab[t][r];
      float e = __builtin_amdgcn_exp2f(B * -1.4426950408889634f);
      y[t][r] = as[t][r] * (B * __builtin_amdgcn_rcpf(1.0f + e)); }); });
    float v[8];
    sfor<4>([&](auto R_) { constexpr int r = R_;
      auto s = __builtin_amdgcn_permlane16_swap(asu(y[0][r]), asu(y[1][r]), false, false);
      v[r] = asf(s[0]); v[4 + r] = asf(s[1]); });
    u32x4 o;
    sfor<4>([&](auto D_) { constexpr int d = D_; o[d] = pk2(bflo(xres[d]) + v[2 * d], bfhi(xres[d]) + v[2 * d + 1]); });
    if constexpr (OST) {
      *(LAS u32x4*)(L + xa_) = o;
      bar();
      const unsigned lo = opq(l), r = 8 * w + (lo >> 3);
      const u32x4 ot = *(LAS const u32x4*)(L + L_XR + r * 128u + (((lo & 7) ^ ((r >> 1) & 7u)) * 16u));
      st128<0>(ot, sOUT, (64u * i + r) * 1024u + j * 128u + (lo & 7) * 16u);
    } else st128<0>(o, sOUT, orow);
  }
}

extern "C" void kernel_launch(void* const* d_in, const int* in_sizes, int n_in, void* d_out, int out_size, void* d_ws,
                              size_t ws_size, hipStream_t stream) {
  (void)in_sizes; (void)n_in; (void)out_size; (void)ws_size;
  (void)hipFuncSetAttribute((const void*)fused_block_kernel, hipFuncAttributeMaxDynamicSharedMemorySize, (int)L_TOTAL);
  fused_block_kernel<<<256, 768, L_TOTAL, stream>>>(d_in[0], d_in[1], d_in[2], d_in[4], d_in[5], d_in[6], d_in[7], d_out, d_ws);
}
```
